```python
import torch

def _softmax(x, axis=-1):
    return torch.softmax(x, dim=axis)

def _gelu(x):
    return torch.nn.functional.gelu(x, approximate='tanh')

def _silu(x):
    return torch.nn.functional.silu(x)

def _sigmoid(x):
    return torch.sigmoid(x)

def _layernorm(x, axis=-1, eps=1e-05):
    m = x.mean(dim=axis, keepdim=True)
    v = x.var(dim=axis, keepdim=True, unbiased=False)
    return (x - m) / torch.sqrt(v + eps)

def _scatter_add(tgt, idx, src, axis=0):
    return tgt.clone().index_add_(axis, idx, src)

def _segment_sum(src, seg, n):
    out = torch.zeros((n,) + src.shape[1:], dtype=src.dtype)
    return out.index_add_(0, seg, src)

def _pad(x, pw):
    flat = tuple((v for pair in reversed(pw) for v in pair))
    return torch.nn.functional.pad(x, flat)
kernel_name = 'standalone__ragged_sequences_varlen_flash_attn__ragged'

def setup_inputs():

    _w = lambda a: (lambda t: t.to(torch.bfloat16) if t.is_floating_point() else t)(torch.as_tensor(a))
    cu = torch.tensor([0, 485, 1609, 2680, 3050, 3769, 5493, 6501, 7684], dtype=torch.int64)
    T = int(cu[-1])
    Hq, Hkv, D = (16, 8, 128)
    assert Hq % Hkv == 0
    return {'q': _w((lambda *s: torch.randn(*s, dtype=torch.bfloat16) * 0.8)(T, Hq, D)), 'k': _w((lambda *s: torch.randn(*s, dtype=torch.bfloat16) * 0.8)(T, Hkv, D)), 'v': _w((lambda *s: torch.randn(*s, dtype=torch.bfloat16) * 0.05)(T, Hkv, D)), 'cu': _w(cu)}

def reference(q, k, v, cu):
    T, Hq, D = q.shape
    Hkv = k.shape[1]
    rep = Hq // Hkv
    scale = D ** (-0.5)
    outs = []
    for i in range(len(cu) - 1):
        s, e = (int(cu[i]), int(cu[i + 1]))
        L = e - s
        qi = q[s:e].clone()
        ki = (lambda x, n, axis: torch.repeat_interleave(x, n, dim=axis))(k[s:e], rep, axis=1)
        vi = (lambda x, n, axis: torch.repeat_interleave(x, n, dim=axis))(v[s:e], rep, axis=1)
        logits = torch.einsum('qhd,khd->hqk', qi, ki) * scale
        ar = torch.arange(L)
        neg = (lambda x, dt: x.to(dt))(ar[None, :] > ar[:, None], logits.dtype) * -1000000000.0
        p = _softmax(logits + neg[None, :, :], axis=-1)
        outs.append(torch.einsum('hqk,khd->qhd', p, vi))
    return torch.concatenate(outs, axis=0)
```

```cpp
#include <hip/hip_runtime.h>
#include <hip/hip_bf16.h>
#include <stdint.h>

typedef short bf16x8 __attribute__((ext_vector_type(8)));
typedef short s16x4 __attribute__((ext_vector_type(4)));
typedef float f32x16 __attribute__((ext_vector_type(16)));
typedef unsigned u32x4 __attribute__((ext_vector_type(4)));
typedef unsigned u32x2 __attribute__((ext_vector_type(2)));
typedef __attribute__((address_space(3))) const char* lds_cptr;
typedef short v4i16_t __attribute__((ext_vector_type(4)));

constexpr int HQ = 16, HKV = 8, D = 128;
constexpr int KVB = 64;
constexpr int QBR = 64;
constexpr int TILE_B = KVB * D * 2;
constexpr int SLOT_B = 2 * TILE_B;
constexpr int LDS_BYTES = 2 * SLOT_B;
constexpr float C2 = 0.08838834764831845f * 1.4426950408889634f;

#define DI __device__ __forceinline__

DI unsigned cvtpk(float lo, float hi) { unsigned r; asm("v_cvt_pk_bf16_f32 %0, %1, %2" : "=v"(r) : "v"(lo), "v"(hi)); return r; }
DI int crow(int r, int hi) { return (r & 3) + 8 * (r >> 2) + 4 * hi; }
DI void glds16(unsigned voff, const void* sbase, unsigned lds_base) {
  asm volatile("s_mov_b32 m0, %2\n\ts_nop 0\n\tglobal_load_lds_dwordx4 %0, %1" :: "v"(voff), "s"(sbase), "s"(lds_base) : "m0", "memory");
}
DI s16x4 vtr(lds_cptr p) { return __builtin_bit_cast(s16x4, __builtin_amdgcn_ds_read_tr16_b64_v4i16((__attribute__((address_space(3))) v4i16_t*)p)); }
DI bf16x8 ldsr128(lds_cptr p) { return *(const __attribute__((address_space(3))) bf16x8*)p; }
#define MFMA(a, b, c) __builtin_amdgcn_mfma_f32_32x32x16_bf16(a, b, c, 0, 0, 0)

__global__ __launch_bounds__(256, 2)
void attn_varlen_fwd(const __hip_bfloat16* __restrict__ Qg, const __hip_bfloat16* __restrict__ Kg, const __hip_bfloat16* __restrict__ Vg,
                     const int* __restrict__ cu, __hip_bfloat16* __restrict__ Og, int nseq) {
  __shared__ __attribute__((aligned(16))) char lds[LDS_BYTES];
  const int tid = threadIdx.x, lane = tid & 63, r32 = lane & 31, hi = lane >> 5;
  const int wid = __builtin_amdgcn_readfirstlane(tid >> 6);
  const int hk = blockIdx.x & 7;
  int rem = blockIdx.x >> 3;

  int seq = -1, qb = 0, s0 = 0, s1 = 0;
  {
    int maxnb = 0;
    for (int s = 0; s < nseq; ++s) { int nb = (cu[s + 1] - cu[s] + QBR - 1) / QBR; maxnb = nb > maxnb ? nb : maxnb; }
    for (int c = maxnb; c >= 1 && seq < 0; --c) {
      for (int s = 0; s < nseq; ++s) {
        int a = cu[s], b = cu[s + 1];
        int nb = (b - a + QBR - 1) / QBR;
        if (nb >= c) { if (rem == 0) { seq = s; qb = c - 1; s0 = a; s1 = b; break; } --rem; }
      }
    }
  }
  if (seq < 0) return;
  s0 = __builtin_amdgcn_readfirstlane(s0); s1 = __builtin_amdgcn_readfirstlane(s1); qb = __builtin_amdgcn_readfirstlane(qb);
  const int L = s1 - s0;
  const int nt = qb + 1;
  const int head = 2 * hk + (wid >> 1);
  const int qrow0 = qb * QBR + 32 * (wid & 1);
  const int qi = qrow0 + r32;
  const int qic = qi < L ? qi : L - 1;

  bf16x8 qr[8];
  {
    const __hip_bfloat16* qp = Qg + ((size_t)(s0 + qic) * HQ + head) * D + hi * 8;
#pragma unroll
    for (int ds = 0; ds < 8; ++ds) qr[ds] = *(const bf16x8*)(qp + ds * 16);
  }

  const unsigned lds0 = (unsigned)(uintptr_t)lds;
  int grow[4]; unsigned kch[4], vch[4];
#pragma unroll
  for (int i = 0; i < 4; ++i) {
    const int r = 16 * wid + 4 * i + (lane >> 4);
    grow[i] = r;
    kch[i] = (unsigned)(((lane & 15) ^ (r & 15)) << 4);
    vch[i] = (unsigned)(((lane & 15) ^ (((r & 3) << 2) | ((r >> 2) & 3))) << 4);
  }
  const char* Kbase = (const char*)Kg + ((size_t)s0 * HKV + hk) * D * 2;
  const char* Vbase = (const char*)Vg + ((size_t)s0 * HKV + hk) * D * 2;
  auto issue_tile = [&](int t, unsigned slot_off) {
    const int lim = L - 1 - t * KVB;
    const char* kb = Kbase + (size_t)t * KVB * (HKV * D * 2);
    const char* vb = Vbase + (size_t)t * KVB * (HKV * D * 2);
#pragma unroll
    for (int i = 0; i < 4; ++i) {
      const int rr = grow[i] < lim ? grow[i] : lim;
      const unsigned ro = (unsigned)rr * (HKV * D * 2);
      glds16(ro + kch[i], kb, __builtin_amdgcn_readfirstlane(lds0 + slot_off + (16 * wid + 4 * i) * 256));
    }
#pragma unroll
    for (int i = 0; i < 4; ++i) {
      const int rr = grow[i] < lim ? grow[i] : lim;
      const unsigned ro = (unsigned)rr * (HKV * D * 2);
      glds16(ro + vch[i], vb, __builtin_amdgcn_readfirstlane(lds0 + slot_off + TILE_B + (16 * wid + 4 * i) * 256));
    }
  };

  unsigned kaddr[8];
#pragma unroll
  for (int ds = 0; ds < 8; ++ds) kaddr[ds] = (unsigned)(r32 * 256 + (((2 * ds + hi) ^ (r32 & 15)) << 4));
  unsigned vaddr[4][2];
  {
    const int q4 = (lane & 15) >> 2, c2 = 2 * ((lane >> 4) & 1) + ((lane & 3) >> 1);
#pragma unroll
    for (int db = 0; db < 4; ++db)
#pragma unroll
      for (int hf = 0; hf < 2; ++hf)
        vaddr[db][hf] = (unsigned)(TILE_B + (8 * hf + 4 * hi + q4) * 256 + (((4 * db + c2) ^ ((q4 << 2) | (2 * hf + hi))) << 4) + 8 * (lane & 1));
  }

  float m_run = -1e30f, l_run = 0.f;
  f32x16 o[4];
#pragma unroll
  for (int db = 0; db < 4; ++db) o[db] = f32x16{};

  issue_tile(0, 0);
#pragma unroll
  for (int ds = 0; ds < 8; ++ds) asm volatile("" : "+v"(qr[ds]));
  unsigned slot = 0;
  for (int t = 0; t < nt; ++t) {
    asm volatile("s_waitcnt vmcnt(0) lgkmcnt(0)\n\ts_barrier" ::: "memory");
    if (t + 1 < nt) issue_tile(t + 1, slot ^ SLOT_B);
    const lds_cptr base = (lds_cptr)lds + slot;
    f32x16 p0 = f32x16{}, p1 = f32x16{};
#pragma unroll
    for (int ds = 0; ds < 8; ++ds) {
      bf16x8 k0 = ldsr128(base + kaddr[ds]);
      bf16x8 k1 = ldsr128(base + kaddr[ds] + 32 * 256);
      p0 = MFMA(k0, qr[ds], p0);
      p1 = MFMA(k1, qr[ds], p1);
    }
    if (t == qb) {
      const float NEG = -__builtin_inff();
      const int dq = qi - t * KVB - 4 * hi;
#pragma unroll
      for (int r = 0; r < 16; ++r) {
        const int c = (r & 3) + 8 * (r >> 2);
        if (c > dq) p0[r] = NEG;
        if (c + 32 > dq) p1[r] = NEG;
      }
    }
    float pmax = p0[0];
#pragma unroll
    for (int r = 1; r < 16; ++r) pmax = fmaxf(pmax, p0[r]);
#pragma unroll
    for (int r = 0; r < 16; ++r) pmax = fmaxf(pmax, p1[r]);
    { auto rr = __builtin_amdgcn_permlane32_swap(__float_as_uint(pmax), __float_as_uint(pmax), false, false);
      pmax = fmaxf(__uint_as_float(rr[0]), __uint_as_float(rr[1])); }
    const float m_new = fmaxf(m_run, pmax);
    const float alpha = __builtin_amdgcn_exp2f((m_run - m_new) * C2);
    m_run = m_new;
    const float nm = -m_new * C2;
    float ps = 0.f;
#pragma unroll
    for (int r = 0; r < 16; ++r) { p0[r] = __builtin_amdgcn_exp2f(fmaf(p0[r], C2, nm)); ps += p0[r]; }
#pragma unroll
    for (int r = 0; r < 16; ++r) { p1[r] = __builtin_amdgcn_exp2f(fmaf(p1[r], C2, nm)); ps += p1[r]; }
    l_run = l_run * alpha + ps;
#pragma unroll
    for (int db = 0; db < 4; ++db)
#pragma unroll
      for (int r = 0; r < 16; ++r) o[db][r] *= alpha;
    bf16x8 pb[4];
    {
      u32x4 w;
      w = (u32x4){cvtpk(p0[0], p0[1]), cvtpk(p0[2], p0[3]), cvtpk(p0[4], p0[5]), cvtpk(p0[6], p0[7])}; pb[0] = __builtin_bit_cast(bf16x8, w);
      w = (u32x4){cvtpk(p0[8], p0[9]), cvtpk(p0[10], p0[11]), cvtpk(p0[12], p0[13]), cvtpk(p0[14], p0[15])}; pb[1] = __builtin_bit_cast(bf16x8, w);
      w = (u32x4){cvtpk(p1[0], p1[1]), cvtpk(p1[2], p1[3]), cvtpk(p1[4], p1[5]), cvtpk(p1[6], p1[7])}; pb[2] = __builtin_bit_cast(bf16x8, w);
      w = (u32x4){cvtpk(p1[8], p1[9]), cvtpk(p1[10], p1[11]), cvtpk(p1[12], p1[13]), cvtpk(p1[14], p1[15])}; pb[3] = __builtin_bit_cast(bf16x8, w);
    }
#pragma unroll
    for (int db = 0; db < 4; ++db) {
#pragma unroll
      for (int s = 0; s < 4; ++s) {
        s16x4 lo = vtr(base + vaddr[db][0] + s * 4096);
        s16x4 hh = vtr(base + vaddr[db][1] + s * 4096);
        bf16x8 va = (bf16x8){lo[0], lo[1], lo[2], lo[3], hh[0], hh[1], hh[2], hh[3]};
        o[db] = MFMA(va, pb[s], o[db]);
      }
    }
    slot ^= SLOT_B;
  }

  {
    auto rr = __builtin_amdgcn_permlane32_swap(__float_as_uint(l_run), __float_as_uint(l_run), false, false);
    l_run = __uint_as_float(rr[0]) + __uint_as_float(rr[1]);
  }
  const float inv = 1.0f / l_run;
  if (qi < L) {
    __hip_bfloat16* op = Og + ((size_t)(s0 + qi) * HQ + head) * D + 4 * hi;
#pragma unroll
    for (int db = 0; db < 4; ++db)
#pragma unroll
      for (int g = 0; g < 4; ++g) {
        u32x2 w = {cvtpk(o[db][4 * g] * inv, o[db][4 * g + 1] * inv), cvtpk(o[db][4 * g + 2] * inv, o[db][4 * g + 3] * inv)};
        *(u32x2*)(op + 32 * db + 8 * g) = w;
      }
  }
}

extern "C" void kernel_launch(void* const* d_in, const int* in_sizes, int n_in,
                              void* d_out, int out_size, void* d_ws, size_t ws_size,
                              hipStream_t stream) {
  const __hip_bfloat16* q = (const __hip_bfloat16*)d_in[0];
  const __hip_bfloat16* k = (const __hip_bfloat16*)d_in[1];
  const __hip_bfloat16* v = (const __hip_bfloat16*)d_in[2];
  const int* cu = (const int*)d_in[3];
  const int nseq = in_sizes[3] - 1;
  const int T = in_sizes[0] / (HQ * D);
  const int nblk = (T + QBR - 1) / QBR + nseq;
  attn_varlen_fwd<<<dim3(8 * nblk), dim3(256), 0, stream>>>(q, k, v, cu, (__hip_bfloat16*)d_out, nseq);
}
```

```cpp
#include <hip/hip_runtime.h>
#include <hip/hip_bf16.h>
#include <stdint.h>

typedef short bf16x8 __attribute__((ext_vector_type(8)));
typedef short s16x4 __attribute__((ext_vector_type(4)));
typedef float f32x16 __attribute__((ext_vector_type(16)));
typedef unsigned u32x4 __attribute__((ext_vector_type(4)));
typedef unsigned u32x2 __attribute__((ext_vector_type(2)));
typedef __attribute__((address_space(3))) const char* lds_cptr;
typedef short v4i16_t __attribute__((ext_vector_type(4)));

constexpr int HQ = 16, HKV = 8, D = 128;
constexpr int KVB = 64;
constexpr int QBR = 64;
constexpr int TILE_B = KVB * D * 2;
constexpr int LDS_K = 0, LDS_V = 2 * TILE_B;
constexpr int LDS_BYTES = 4 * TILE_B;
constexpr float C2 = 0.08838834764831845f * 1.4426950408889634f;
constexpr float THR = 8.f;

#define DI __device__ __forceinline__
#define SBAR() __builtin_amdgcn_sched_barrier(0)
#define PIN(x) asm volatile("" : "+v"(x))
#define MFMA(a, b, c) __builtin_amdgcn_mfma_f32_32x32x16_bf16(a, b, c, 0, 0, 0)

DI unsigned cvtpk(float lo, float hi) { unsigned r; asm("v_cvt_pk_bf16_f32 %0, %1, %2" : "=v"(r) : "v"(lo), "v"(hi)); return r; }
DI void glds16(unsigned voff, const void* sbase, unsigned lds_base) {
  asm volatile("s_mov_b32 m0, %2\n\ts_nop 0\n\tglobal_load_lds_dwordx4 %0, %1" :: "v"(voff), "s"(sbase), "s"(lds_base) : "m0", "memory");
}
DI s16x4 vtr(lds_cptr p) { return __builtin_bit_cast(s16x4, __builtin_amdgcn_ds_read_tr16_b64_v4i16((__attribute__((address_space(3))) v4i16_t*)p)); }
DI bf16x8 ldsr128(lds_cptr p) { return *(const __attribute__((address_space(3))) bf16x8*)p; }
#define MX3(a, b, c) __builtin_fmaxf(__builtin_fmaxf((a), (b)), (c))
DI float rowmax(const f32x16& p0, const f32x16& p1) {
  float a = MX3(p0[0], p0[1], p1[0]), b = MX3(p0[2], p0[3], p1[1]); a = MX3(a, p1[2], p1[3]);
#pragma unroll
  for (int r = 4; r < 16; r += 4) { a = MX3(a, p0[r], p0[r + 1]); b = MX3(b, p0[r + 2], p0[r + 3]); a = MX3(a, p1[r], p1[r + 1]); b = MX3(b, p1[r + 2], p1[r + 3]); }
  float m = __builtin_fmaxf(a, b); auto rr = __builtin_amdgcn_permlane32_swap(__float_as_uint(m), __float_as_uint(m), false, false);
  return __builtin_fmaxf(__uint_as_float(rr[0]), __uint_as_float(rr[1]));
}

__global__ __launch_bounds__(256, 2)
void attn_varlen_fwd(const __hip_bfloat16* __restrict__ Qg, const __hip_bfloat16* __restrict__ Kg, const __hip_bfloat16* __restrict__ Vg,
                     const int* __restrict__ cu, __hip_bfloat16* __restrict__ Og, int nseq) {
  __shared__ __attribute__((aligned(16))) char lds[LDS_BYTES];
  const int tid = threadIdx.x, lane = tid & 63, r32 = lane & 31, hi = lane >> 5;
  const int wid = __builtin_amdgcn_readfirstlane(tid >> 6);
  const int hk = blockIdx.x & 7;
  int rem = blockIdx.x >> 3;

  int seq = -1, qb = 0, s0 = 0, s1 = 0;
  if (nseq <= 64) {
    int a_l = 0, b_l = 0;
    if (lane < nseq) { a_l = cu[lane]; b_l = cu[lane + 1]; }
    const int nb_l = (b_l - a_l + QBR - 1) / QBR;
    int nbm = nb_l;
#pragma unroll
    for (int off = 32; off >= 1; off >>= 1) { int o2 = __shfl_xor(nbm, off); nbm = o2 > nbm ? o2 : nbm; }
    const int maxnb = __builtin_amdgcn_readfirstlane(nbm);
    for (int c = maxnb; c >= 1; --c) {
      unsigned long long m = __ballot(nb_l >= c);
      const int cnt = __popcll(m);
      if (rem < cnt) {
        for (int i = 0; i < rem; ++i) m &= m - 1;
        seq = __builtin_ctzll(m); qb = c - 1;
        s0 = __builtin_amdgcn_readlane(a_l, seq); s1 = __builtin_amdgcn_readlane(b_l, seq);
        break;
      }
      rem -= cnt;
    }
  } else {
    int maxnb = 0;
    for (int s = 0; s < nseq; ++s) { int nb = (cu[s + 1] - cu[s] + QBR - 1) / QBR; maxnb = nb > maxnb ? nb : maxnb; }
    for (int c = maxnb; c >= 1 && seq < 0; --c) {
      for (int s = 0; s < nseq; ++s) {
        int a = cu[s], b = cu[s + 1];
        int nb = (b - a + QBR - 1) / QBR;
        if (nb >= c) { if (rem == 0) { seq = s; qb = c - 1; s0 = a; s1 = b; break; } --rem; }
      }
    }
  }
  if (seq < 0) return;
  s0 = __builtin_amdgcn_readfirstlane(s0); s1 = __builtin_amdgcn_readfirstlane(s1); qb = __builtin_amdgcn_readfirstlane(qb);
  const int L = s1 - s0;
  const int nt = qb + 1;
  const int head = 2 * hk + (wid >> 1);
  const int qi = qb * QBR + 32 * (wid & 1) + r32;
  const int qic = qi < L ? qi : L - 1;

  const unsigned lds0 = (unsigned)(uintptr_t)lds;
  unsigned koff[4], voff[4];
#pragma unroll
  for (int i = 0; i < 4; ++i) {
    const int r = 16 * wid + 4 * i + (lane >> 4);
    koff[i] = (unsigned)(r * (HKV * D * 2) + (((lane & 15) ^ (r & 15)) << 4));
    voff[i] = (unsigned)(r * (HKV * D * 2) + (((lane & 15) ^ (((r & 3) << 2) | ((r >> 2) & 3))) << 4));
  }
  const char* Kbase = (const char*)Kg + ((size_t)s0 * HKV + hk) * D * 2;
  const char* Vbase = (const char*)Vg + ((size_t)s0 * HKV + hk) * D * 2;
  auto issue_kv = [&](int t, const char* base, const unsigned* off, unsigned slot) {
    const int lim = L - 1 - t * KVB;
    const char* gb = base + (size_t)t * KVB * (HKV * D * 2);
    if (lim >= KVB - 1) {
#pragma unroll
      for (int i = 0; i < 4; ++i) glds16(off[i], gb, __builtin_amdgcn_readfirstlane(lds0 + slot + (16 * wid + 4 * i) * 256));
    } else {
#pragma unroll
      for (int i = 0; i < 4; ++i) {
        const int r = 16 * wid + 4 * i + (lane >> 4); const int dr = r < lim ? 0 : (r - lim) * (HKV * D * 2);
        glds16(off[i] - dr, gb, __builtin_amdgcn_readfirstlane(lds0 + slot + (16 * wid + 4 * i) * 256));
      }
    }
  };
  issue_kv(0, Kbase, koff, LDS_K);

  bf16x8 qr[8];
  {
    const __hip_bfloat16* qp = Qg + ((size_t)(s0 + qic) * HQ + head) * D + hi * 8;
#pragma unroll
    for (int ds = 0; ds < 8; ++ds) qr[ds] = *(const bf16x8*)(qp + ds * 16);
  }
#pragma unroll
  for (int ds = 0; ds < 8; ++ds) PIN(qr[ds]);

  unsigned kaddr[8];
#pragma unroll
  for (int ds = 0; ds < 8; ++ds) kaddr[ds] = lds0 + (unsigned)(LDS_K + r32 * 256 + (((2 * ds + hi) ^ (r32 & 15)) << 4));
  unsigned vaddr[4][2];
  {
    const int q4 = (lane & 15) >> 2, c2 = 2 * ((lane >> 4) & 1) + ((lane & 3) >> 1);
#pragma unroll
    for (int db = 0; db < 4; ++db)
#pragma unroll
      for (int hf = 0; hf < 2; ++hf)
        vaddr[db][hf] = lds0 + (unsigned)(LDS_V + (8 * hf + 4 * hi + q4) * 256 + (((4 * db + c2) ^ ((q4 << 2) | (2 * hf + hi))) << 4) + 8 * (lane & 1));
  }

  float mhat, l_run = 0.f;
  f32x16 o[4];
#pragma unroll
  for (int db = 0; db < 4; ++db) o[db] = f32x16{};
  f32x16 pA0, pA1, pB0, pB1;
  bf16x8 kf[4]; s16x4 vlo[4], vhi[4]; u32x4 pw[4];
  unsigned kslot = 0;

#define KRD(j) kf[(j) & 3] = ldsr128((lds_cptr)(uintptr_t)(kaddr[(j) >> 1] + kslot) + ((j) & 1) * 8192)
#define CMASK(X0, X1, t) do { const float NEG = -__builtin_inff(); const int dq = qi - (t) * KVB - 4 * hi; \
    _Pragma("unroll") for (int r = 0; r < 16; ++r) { const int c = (r & 3) + 8 * (r >> 2); if (c > dq) X0[r] = NEG; if (c + 32 > dq) X1[r] = NEG; } } while (0)

  asm volatile("s_waitcnt vmcnt(0) lgkmcnt(0)\n\ts_barrier" ::: "memory");
  if (nt > 1) issue_kv(1, Kbase, koff, LDS_K + TILE_B);
  issue_kv(0, Vbase, voff, LDS_V);
  {
    pA0 = f32x16{}; pA1 = f32x16{};
    KRD(0); KRD(1); KRD(2);
#pragma unroll
    for (int j = 0; j < 16; ++j) {
      if (j + 3 < 16) KRD(j + 3);
      if (j & 1) pA1 = MFMA(kf[j & 3], qr[j >> 1], pA1); else pA0 = MFMA(kf[j & 3], qr[j >> 1], pA0);
    }
    if (nt == 1) CMASK(pA0, pA1, 0);
    mhat = rowmax(pA0, pA1) * C2;
    const float nmh = -mhat;
#pragma unroll
    for (int r = 0; r < 16; ++r) { pA0[r] = __builtin_amdgcn_exp2f(fmaf(pA0[r], C2, nmh)); pA1[r] = __builtin_amdgcn_exp2f(fmaf(pA1[r], C2, nmh)); }
  }
  kslot ^= TILE_B;

#define PIDX(P0, P1, i) ((i) < 16 ? P0[(i) & 15] : P1[(i) & 15])
#define VRD(i) do { const int db_ = (i) >> 2, s_ = (i) & 3; \
    vlo[(i) & 3] = vtr((lds_cptr)(uintptr_t)(vaddr[db_][0] + vsl) + s_ * 4096); vhi[(i) & 3] = vtr((lds_cptr)(uintptr_t)(vaddr[db_][1] + vsl) + s_ * 4096); } while (0)
#define VFR(i) (bf16x8){vlo[(i) & 3][0], vlo[(i) & 3][1], vlo[(i) & 3][2], vlo[(i) & 3][3], vhi[(i) & 3][0], vhi[(i) & 3][1], vhi[(i) & 3][2], vhi[(i) & 3][3]}
#define STEP(C0, C1, P0, P1, t) do { \
    asm volatile("s_waitcnt vmcnt(0) lgkmcnt(0)\n\ts_barrier" ::: "memory"); \
    if ((t) + 1 < nt) issue_kv((t) + 1, Kbase, koff, LDS_K + (kslot ^ TILE_B)); \
    issue_kv((t), Vbase, voff, LDS_V + kslot); \
    SBAR(); \
    KRD(0); KRD(1); KRD(2); float sacc = 0.f; C0 = f32x16{}; C1 = f32x16{}; SBAR(); \
    _Pragma("unroll") for (int j = 0; j < 16; ++j) { \
      if (j + 3 < 16) KRD(j + 3); \
      if (j & 1) { C1 = MFMA(kf[j & 3], qr[j >> 1], C1); PIN(C1); } else { C0 = MFMA(kf[j & 3], qr[j >> 1], C0); PIN(C0); } \
      sacc += PIDX(P0, P1, 2 * j); sacc += PIDX(P0, P1, 2 * j + 1); \
      pw[j >> 2][j & 3] = cvtpk(PIDX(P0, P1, 2 * j), PIDX(P0, P1, 2 * j + 1)); \
      PIN(sacc); PIN(pw[j >> 2]); SBAR(); } \
    l_run += sacc; \
    if ((t) == nt - 1) CMASK(C0, C1, (t)); \
    float fsc = 1.f; bool resc = false; \
    { const float rm = fmaf(rowmax(C0, C1), C2, -mhat); \
      if (__builtin_expect(__any(rm > THR), 0)) { const float dl = fmaxf(rm, 0.f); mhat += dl; fsc = __builtin_amdgcn_exp2f(-dl); l_run *= fsc; resc = true; } } \
    const float nmh = -mhat; const unsigned vsl = kslot ^ TILE_B; \
    VRD(0); VRD(1); VRD(2); SBAR(); \
    _Pragma("unroll") for (int j = 0; j < 16; ++j) { \
      if (j + 3 < 16) VRD(j + 3); \
      o[j >> 2] = MFMA(VFR(j), __builtin_bit_cast(bf16x8, pw[j & 3]), o[j >> 2]); PIN(o[j >> 2]); \
      if (j < 8) { C0[2 * j] = __builtin_amdgcn_exp2f(fmaf(C0[2 * j], C2, nmh)); C0[2 * j + 1] = __builtin_amdgcn_exp2f(fmaf(C0[2 * j + 1], C2, nmh)); PIN(C0); } \
      else { C1[2 * j - 16] = __builtin_amdgcn_exp2f(fmaf(C1[2 * j - 16], C2, nmh)); C1[2 * j - 15] = __builtin_amdgcn_exp2f(fmaf(C1[2 * j - 15], C2, nmh)); PIN(C1); } \
      SBAR(); } \
    if (resc) { _Pragma("unroll") for (int db = 0; db < 4; ++db) _Pragma("unroll") for (int r = 0; r < 16; ++r) o[db][r] *= fsc; } \
    kslot ^= TILE_B; \
  } while (0)

  int t = 1;
  for (; t + 1 < nt; t += 2) {
    STEP(pB0, pB1, pA0, pA1, t);
    STEP(pA0, pA1, pB0, pB1, t + 1);
  }
  if (t < nt) { STEP(pB0, pB1, pA0, pA1, t); pA0 = pB0; pA1 = pB1; }

  asm volatile("s_waitcnt vmcnt(0) lgkmcnt(0)\n\ts_barrier" ::: "memory");
  {
    float sacc = 0.f;
#pragma unroll
    for (int r = 0; r < 16; ++r) sacc += pA0[r] + pA1[r];
    l_run += sacc;
#pragma unroll
    for (int j = 0; j < 16; ++j) pw[j >> 2][j & 3] = cvtpk(PIDX(pA0, pA1, 2 * j), PIDX(pA0, pA1, 2 * j + 1));
    const unsigned vsl = kslot ^ TILE_B;
    VRD(0); VRD(1); VRD(2);
#pragma unroll
    for (int j = 0; j < 16; ++j) {
      if (j + 3 < 16) VRD(j + 3);
      o[j >> 2] = MFMA(VFR(j), __builtin_bit_cast(bf16x8, pw[j & 3]), o[j >> 2]);
    }
  }

  {
    auto rr = __builtin_amdgcn_permlane32_swap(__float_as_uint(l_run), __float_as_uint(l_run), false, false);
    l_run = __uint_as_float(rr[0]) + __uint_as_float(rr[1]);
  }
  const float inv = 1.0f / l_run;
  if (qi < L) {
    __hip_bfloat16* op = Og + ((size_t)(s0 + qi) * HQ + head) * D + 4 * hi;
#pragma unroll
    for (int db = 0; db < 4; ++db)
#pragma unroll
      for (int g = 0; g < 4; ++g) {
        u32x2 w = {cvtpk(o[db][4 * g] * inv, o[db][4 * g + 1] * inv), cvtpk(o[db][4 * g + 2] * inv, o[db][4 * g + 3] * inv)};
        *(u32x2*)(op + 32 * db + 8 * g) = w;
      }
  }
}

extern "C" void kernel_launch(void* const* d_in, const int* in_sizes, int n_in,
                              void* d_out, int out_size, void* d_ws, size_t ws_size,
                              hipStream_t stream) {
  const __hip_bfloat16* q = (const __hip_bfloat16*)d_in[0];
  const __hip_bfloat16* k = (const __hip_bfloat16*)d_in[1];
  const __hip_bfloat16* v = (const __hip_bfloat16*)d_in[2];
  const int* cu = (const int*)d_in[3];
  const int nseq = in_sizes[3] - 1;
  const int T = in_sizes[0] / (HQ * D);
  const int nblk = (T + QBR - 1) / QBR + nseq;
  attn_varlen_fwd<<<dim3(8 * nblk), dim3(256), 0, stream>>>(q, k, v, cu, (__hip_bfloat16*)d_out, nseq);
}
```
